```python
import torch

def _softmax(x, axis=-1):
    return torch.softmax(x, dim=axis)
def _gelu(x):
    return torch.nn.functional.gelu(x, approximate="tanh")
def _silu(x):
    return torch.nn.functional.silu(x)
def _sigmoid(x):
    return torch.sigmoid(x)
def _layernorm(x, axis=-1, eps=1e-5):
    m = x.mean(dim=axis, keepdim=True)
    v = x.var(dim=axis, keepdim=True, unbiased=False)
    return (x - m) / torch.sqrt(v + eps)
def _scatter_add(tgt, idx, src, axis=0):
    return tgt.clone().index_add_(axis, idx, src)
def _segment_sum(src, seg, n):
    out = torch.zeros((n,) + src.shape[1:], dtype=src.dtype)
    return out.index_add_(0, seg, src)
def _pad(x, pw):
    flat = tuple(v for pair in reversed(pw) for v in pair)
    return torch.nn.functional.pad(x, flat)

kernel_name = 'standalone__cross_network_dcn__recsys'

def setup_inputs():

    _w = (lambda a: (lambda t: t.to(torch.bfloat16) if t.is_floating_point() else t)(torch.as_tensor(a)))
    B, F, D, L = 16384, 26, 32, 3
    Din = min(F * D, 1024)
    return {
        "x0": _w((lambda *s: torch.randn(*s, dtype=torch.bfloat16) * 0.05)(B, Din)),
        "ws": _w((lambda *s: torch.randn(*s, dtype=torch.bfloat16) * 0.4)(L, Din, Din) * (Din ** -0.5)),
        "bs": _w((lambda *s: torch.randn(*s, dtype=torch.bfloat16) * 3.2)(L, Din) * 0.01),
    }

def reference(x0, ws, bs):
    x = x0
    L = ws.shape[0]
    for l in range(L):
        proj = torch.einsum('bd,de->be', x, ws[l]) + bs[l]
        x = x0 * proj + x
    return x
```

```cpp
#include <hip/hip_runtime.h>
#include <hip/hip_bf16.h>

typedef short bf16x8 __attribute__((ext_vector_type(8)));
typedef float f32x4 __attribute__((ext_vector_type(4)));
typedef unsigned short u16;
typedef unsigned short u16x4 __attribute__((ext_vector_type(4)));

constexpr int BATCH = 16384;
constexpr int DIN = 832;
constexpr int NLAYER = 3;
constexpr int BM = 64;
constexpr int KS = DIN / 32;
constexpr int NT = DIN / 16;
constexpr int NW = 13;
constexpr int KC = DIN / 8;
constexpr int TOTAL_STEPS = NLAYER * KS;

__global__ __launch_bounds__(256) void retile_w_kernel(const u16* __restrict__ W, bf16x8* __restrict__ Wt) {
    const int idx = blockIdx.x * 256 + threadIdx.x;
    const int total = NLAYER * KS * NT * 64;
    if (idx >= total) return;
    const int lane = idx & 63;
    const int frag = idx >> 6;
    const int nt = frag % NT;
    const int lks = frag / NT;
    const int l = lks / KS, ks = lks % KS;
    const u16* src = W + (size_t)l * DIN * DIN + (size_t)(ks * 32 + 8 * (lane >> 4)) * DIN + nt * 16 + (lane & 15);
    bf16x8 v;
#pragma unroll
    for (int j = 0; j < 8; ++j) v[j] = (short)src[(size_t)j * DIN];
    Wt[idx] = v;
}

__device__ __forceinline__ float bf2f(u16 u) { return __uint_as_float(((unsigned)u) << 16); }
__device__ __forceinline__ u16 f2bf(float f) {
    __hip_bfloat16 h = __float2bfloat16(f);
    return *reinterpret_cast<u16*>(&h);
}
__device__ __forceinline__ float rbf(float f) { return bf2f(f2bf(f)); }

__global__ __launch_bounds__(256, 1) void dcn_fused_kernel(const u16* __restrict__ x0, const bf16x8* __restrict__ Wt,
                                                           const u16* __restrict__ bs, u16* __restrict__ out) {
    __shared__ __attribute__((aligned(16))) char smem[KC * BM * 16];
    const int tid = threadIdx.x, lane = tid & 63, wave = tid >> 6;
    const int row0 = blockIdx.x * BM;
    const int fr = lane & 15, fq = lane >> 4;

    {
        const u16* src = x0 + (size_t)(row0 + lane) * DIN;
        for (int kc = wave; kc < KC; kc += 4) {
            __builtin_amdgcn_global_load_lds((const unsigned*)(src + kc * 8), (unsigned*)(smem + kc * 1024), 16, 0, 0);
        }
    }

    const bf16x8* wp = Wt + (size_t)wave * NW * 64 + lane;
    bf16x8 ring[2][NW];
#pragma unroll
    for (int h = 0; h < 2; ++h)
#pragma unroll
        for (int j = 0; j < NW; ++j) ring[h][j] = wp[(size_t)h * NT * 64 + j * 64];

    asm volatile("s_waitcnt vmcnt(0)" ::: "memory");
    __syncthreads();

    const char* abase = smem + fq * 1024 + fr * 16;

    for (int l = 0; l < NLAYER; ++l) {
        f32x4 acc[4][NW];
#pragma unroll
        for (int i = 0; i < 4; ++i)
#pragma unroll
            for (int j = 0; j < NW; ++j) acc[i][j] = (f32x4){0.f, 0.f, 0.f, 0.f};

        asm volatile("s_nop 4" ::: "memory");
        bf16x8 a[2][4];
#pragma unroll
        for (int i = 0; i < 4; ++i) a[0][i] = *(const bf16x8*)(abase + i * 256);

#pragma unroll 1
        for (int it = 0; it < KS / 2; ++it) {
#pragma unroll
            for (int h = 0; h < 2; ++h) {
                const int ks = 2 * it + h;
                if (ks + 1 < KS) {
#pragma unroll
                    for (int i = 0; i < 4; ++i) a[h ^ 1][i] = *(const bf16x8*)(abase + (ks + 1) * 4096 + i * 256);
                }
                int gnext = l * KS + ks + 2;
                gnext = gnext < TOTAL_STEPS ? gnext : TOTAL_STEPS - 1;
                const bf16x8* wn = wp + (size_t)gnext * NT * 64;
#pragma unroll
                for (int j = 0; j < NW; ++j) {
#pragma unroll
                    for (int i = 0; i < 4; ++i)
                        asm volatile("v_mfma_f32_16x16x32_bf16 %0, %1, %2, %0" : "+a"(acc[i][j]) : "v"(ring[h][j]), "v"(a[h][i]));
                    ring[h][j] = wn[j * 64];
                }
            }
        }

        asm volatile("s_nop 7\n\ts_nop 7" ::: "memory");
        __syncthreads();

#pragma unroll
        for (int j = 0; j < NW; ++j) {
            const int n = wave * (NW * 16) + 16 * j + 4 * fq;
            const u16x4 b4 = *(const u16x4*)(bs + l * DIN + n);
#pragma unroll
            for (int i = 0; i < 4; ++i) {
                const int row = 16 * i + fr;
                const u16x4 x04 = *(const u16x4*)(x0 + (size_t)(row0 + row) * DIN + n);
                char* p = smem + (n >> 3) * 1024 + row * 16 + (n & 7) * 2;
                const u16x4 xl4 = *(const u16x4*)p;
                u16x4 o4;
#pragma unroll
                for (int r = 0; r < 4; ++r) {
                    float pr = rbf(acc[i][j][r]);
                    pr = rbf(pr + bf2f(b4[r]));
                    float t = rbf(bf2f(x04[r]) * pr);
                    o4[r] = f2bf(t + bf2f(xl4[r]));
                }
                if (l < NLAYER - 1) *(u16x4*)p = o4;
                else *(u16x4*)(out + (size_t)(row0 + row) * DIN + n) = o4;
            }
        }
        __syncthreads();
    }
}

extern "C" void kernel_launch(void* const* d_in, const int* in_sizes, int n_in,
                              void* d_out, int out_size, void* d_ws, size_t ws_size,
                              hipStream_t stream) {
    const u16* x0 = (const u16*)d_in[0];
    const u16* ws = (const u16*)d_in[1];
    const u16* bs = (const u16*)d_in[2];
    u16* out = (u16*)d_out;
    bf16x8* Wt = (bf16x8*)d_ws;
    const int total = NLAYER * KS * NT * 64;
    retile_w_kernel<<<(total + 255) / 256, 256, 0, stream>>>(ws, Wt);
    dcn_fused_kernel<<<BATCH / BM, 256, 0, stream>>>(x0, Wt, bs, out);
}
```

```cpp
#include <hip/hip_runtime.h>
#include <hip/hip_bf16.h>

typedef short bf16x8 __attribute__((ext_vector_type(8)));
typedef float f32x4 __attribute__((ext_vector_type(4)));
typedef unsigned short u16;
typedef unsigned short u16x4 __attribute__((ext_vector_type(4)));

constexpr int BATCH = 16384;
constexpr int DIN = 832;
constexpr int NLAYER = 3;
constexpr int BM = 64;
constexpr int KS = DIN / 32;
constexpr int NT = DIN / 16;
constexpr int NW = 13;
constexpr int NWV = 6;
constexpr int NX0A = 0;
constexpr int NWA = NW - NWV;
constexpr int KC = DIN / 8;
constexpr int XS_BYTES = KC * BM * 16;
constexpr int LDS_BYTES = XS_BYTES + 4 * NWA * 4 * 512;
constexpr int TOTAL_STEPS = NLAYER * KS;

__global__ __launch_bounds__(256) void retile_w_kernel(const u16* __restrict__ W, bf16x8* __restrict__ Wt) {
    const int idx = blockIdx.x * 256 + threadIdx.x;
    const int total = NLAYER * KS * NT * 64;
    if (idx >= total) return;
    const int lane = idx & 63;
    const int frag = idx >> 6;
    const int nt = frag % NT;
    const int lks = frag / NT;
    const int l = lks / KS, ks = lks % KS;
    const u16* src = W + (size_t)l * DIN * DIN + (size_t)(ks * 32 + 8 * (lane >> 4)) * DIN + nt * 16 + (lane & 15);
    bf16x8 v;
#pragma unroll
    for (int j = 0; j < 8; ++j) v[j] = (short)src[(size_t)j * DIN];
    Wt[idx] = v;
}

__device__ __forceinline__ float bf2f(u16 u) { return __uint_as_float(((unsigned)u) << 16); }
__device__ __forceinline__ u16 f2bf(float f) {
    __hip_bfloat16 h = __float2bfloat16(f);
    return *reinterpret_cast<u16*>(&h);
}
__device__ __forceinline__ float rbf(float f) { return bf2f(f2bf(f)); }

template <bool LAST>
__device__ __forceinline__ void epilogue(f32x4 (&acc)[4][NW], const unsigned (&x0r)[NWV][4][2], const u16* __restrict__ x0g,
                                         u16* __restrict__ outr, char* smem) {
    int tz = threadIdx.x;
    asm volatile("" : "+v"(tz));
    const int lane = tz & 63, wave = tz >> 6, fr = tz & 15, fq = (tz >> 4) & 3;
    const char* x0l = smem + XS_BYTES + wave * (NWA * 4 * 512) + lane * 8;
    const int nb = wave * (NW * 16) + 4 * fq;
    char* lp = smem + (nb >> 3) * 1024 + fr * 16 + (nb & 7) * 2;
    u16* op = outr + (size_t)fr * DIN + nb;
    uint2 xg[4];
#pragma unroll
    for (int i = 0; i < 4; ++i) xg[i] = *(const uint2*)(x0g + (size_t)(16 * i + fr) * DIN + nb + 16 * (NWV - 1));
    asm volatile("s_nop 7\n\ts_nop 7" ::: "memory");
    __syncthreads();
#pragma unroll
    for (int j = 0; j < NW; ++j) {
#pragma unroll
        for (int i = 0; i < 4; ++i) {
            uint2 xq;
            if (j == NWV - 1) xq = xg[i];
            else if (j >= NX0A && j < NWV) { xq.x = x0r[j][i][0]; xq.y = x0r[j][i][1]; asm volatile("" : "+v"(xq.x), "+v"(xq.y)); }
            else if (j < NWV) {
                asm volatile("v_accvgpr_read_b32 %0, %1" : "=v"(xq.x) : "a"(x0r[j][i][0]));
                asm volatile("v_accvgpr_read_b32 %0, %1" : "=v"(xq.y) : "a"(x0r[j][i][1]));
            }
            else xq = *(const uint2*)(x0l + ((j - NWV) * 4 + i) * 512);
            const float x0f[4] = {__uint_as_float(xq.x << 16), __uint_as_float(xq.x & 0xffff0000u),
                                  __uint_as_float(xq.y << 16), __uint_as_float(xq.y & 0xffff0000u)};
            char* p = lp + j * 2048 + i * 256;
            const u16x4 xl4 = *(const u16x4*)p;
            u16x4 o4;
#pragma unroll
            for (int r = 0; r < 4; ++r) o4[r] = f2bf(__builtin_fmaf(x0f[r], acc[i][j][r], bf2f(xl4[r])));
            if (!LAST) *(u16x4*)p = o4;
            else *(u16x4*)(op + (size_t)(16 * i) * DIN + 16 * j) = o4;
        }
        __builtin_amdgcn_sched_barrier(0);
    }
}

__global__ __launch_bounds__(256, 1) void dcn_fused_kernel(const u16* __restrict__ x0, const bf16x8* __restrict__ Wt,
                                                           const u16* __restrict__ bs, u16* __restrict__ out) {
    __shared__ __attribute__((aligned(16))) char smem[LDS_BYTES];
    const int tid = threadIdx.x, lane = tid & 63, wave = tid >> 6;
    const int row0 = blockIdx.x * BM;
    const int fr = lane & 15, fq = lane >> 4;

    {
        const u16* src = x0 + (size_t)(row0 + lane) * DIN;
        for (int kc = wave; kc < KC; kc += 4) {
            __builtin_amdgcn_global_load_lds((const unsigned*)(src + kc * 8), (unsigned*)(smem + kc * 1024), 16, 0, 0);
        }
    }

    const bf16x8* wp = Wt + (size_t)wave * NW * 64 + lane;
    bf16x8 ring[2][NW];
#pragma unroll
    for (int h = 0; h < 2; ++h)
#pragma unroll
        for (int j = 0; j < NW; ++j) ring[h][j] = wp[(size_t)h * NT * 64 + j * 64];

    asm volatile("s_waitcnt vmcnt(0)" ::: "memory");
    __syncthreads();


    unsigned x0r[NWV][4][2];
    {
        const int nb = wave * (NW * 16) + 4 * fq;
        const char* lp = smem + (nb >> 3) * 1024 + fr * 16 + (nb & 7) * 2;
#pragma unroll
        for (int j = 0; j < NW; ++j)
#pragma unroll
            for (int i = 0; i < 4; ++i) {
                const uint2 t = *(const uint2*)(lp + j * 2048 + i * 256);
                if (j == NWV - 1) {}
                else if (j >= NX0A && j < NWV) { x0r[j][i][0] = t.x; x0r[j][i][1] = t.y; }
                else if (j < NWV) {
                    asm volatile("v_accvgpr_write_b32 %0, %1" : "=a"(x0r[j][i][0]) : "v"(t.x));
                    asm volatile("v_accvgpr_write_b32 %0, %1" : "=a"(x0r[j][i][1]) : "v"(t.y));
                }
                else *(uint2*)(smem + XS_BYTES + wave * (NWA * 4 * 512) + ((j - NWV) * 4 + i) * 512 + lane * 8) = t;
            }
    }

    for (int l = 0; l < NLAYER; ++l) {
        f32x4 acc[4][NW];
        f32x4 bias[NW];
        {
            int tz = threadIdx.x;
            asm volatile("" : "+v"(tz));
            const u16* bp = bs + l * DIN + (tz >> 6) * (NW * 16) + 4 * ((tz >> 4) & 3);
#pragma unroll
            for (int j = 0; j < NW; ++j) {
                const u16x4 b4 = *(const u16x4*)(bp + 16 * j);
                bias[j] = (f32x4){bf2f(b4[0]), bf2f(b4[1]), bf2f(b4[2]), bf2f(b4[3])};
            }
        }
#pragma unroll
        for (int i = 0; i < 4; ++i)
#pragma unroll
            for (int j = 0; j < NW; ++j) acc[i][j] = bias[j];

        asm volatile("s_nop 4" ::: "memory");
        int tq = threadIdx.x;
        asm volatile("" : "+v"(tq));
        const char* abase = smem + ((tq >> 4) & 3) * 1024 + (tq & 15) * 16;
        bf16x8 a[2][4];
#pragma unroll
        for (int i = 0; i < 4; ++i) a[0][i] = *(const bf16x8*)(abase + i * 256);

#pragma unroll 1
        for (int it = 0; it < KS / 2; ++it) {
#pragma unroll
            for (int h = 0; h < 2; ++h) {
                const int ks = 2 * it + h;
                if (ks + 1 < KS) {
#pragma unroll
                    for (int i = 0; i < 4; ++i) a[h ^ 1][i] = *(const bf16x8*)(abase + (ks + 1) * 4096 + i * 256);
                }
                int gnext = l * KS + ks + 2;
                gnext = gnext < TOTAL_STEPS ? gnext : TOTAL_STEPS - 1;
                const bf16x8* wn = wp + (size_t)gnext * NT * 64;
#pragma unroll
                for (int j = 0; j < NW; ++j) {
#pragma unroll
                    for (int i = 0; i < 4; ++i)
                        asm volatile("v_mfma_f32_16x16x32_bf16 %0, %1, %2, %0" : "+a"(acc[i][j]) : "v"(ring[h][j]), "v"(a[h][i]));
                    ring[h][j] = wn[j * 64];
                }
            }
        }


        if (l < NLAYER - 1) epilogue<false>(acc, x0r, x0 + (size_t)row0 * DIN, out, smem);
        else epilogue<true>(acc, x0r, x0 + (size_t)row0 * DIN, out + (size_t)row0 * DIN, smem);
        __syncthreads();
    }
}

extern "C" void kernel_launch(void* const* d_in, const int* in_sizes, int n_in,
                              void* d_out, int out_size, void* d_ws, size_t ws_size,
                              hipStream_t stream) {
    const u16* x0 = (const u16*)d_in[0];
    const u16* ws = (const u16*)d_in[1];
    const u16* bs = (const u16*)d_in[2];
    u16* out = (u16*)d_out;
    bf16x8* Wt = (bf16x8*)d_ws;
    const int total = NLAYER * KS * NT * 64;
    retile_w_kernel<<<(total + 255) / 256, 256, 0, stream>>>(ws, Wt);
    dcn_fused_kernel<<<BATCH / BM, 256, 0, stream>>>(x0, Wt, bs, out);
}
```
